```python
import torch

def _softmax(x, axis=-1):
    return torch.softmax(x, dim=axis)
def _gelu(x):
    return torch.nn.functional.gelu(x, approximate="tanh")
def _silu(x):
    return torch.nn.functional.silu(x)
def _sigmoid(x):
    return torch.sigmoid(x)
def _layernorm(x, axis=-1, eps=1e-5):
    m = x.mean(dim=axis, keepdim=True)
    v = x.var(dim=axis, keepdim=True, unbiased=False)
    return (x - m) / torch.sqrt(v + eps)
def _scatter_add(tgt, idx, src, axis=0):
    return tgt.clone().index_add_(axis, idx, src)
def _segment_sum(src, seg, n):
    out = torch.zeros((n,) + src.shape[1:], dtype=src.dtype)
    return out.index_add_(0, seg, src)
def _pad(x, pw):
    flat = tuple(v for pair in reversed(pw) for v in pair)
    return torch.nn.functional.pad(x, flat)

kernel_name = 'standalone__fused_io_aware_attn__prompt'

def setup_inputs():

    _w = (lambda a: (lambda t: t.to(torch.bfloat16) if t.is_floating_point() else t)(torch.as_tensor(a)))
    B, H, S = 2, 32, 2048
    Dh = max(1, 512 // 32)
    return {
        "q": _w((lambda *s: torch.randn(*s, dtype=torch.bfloat16) * 0.8)(B, H, S, Dh)),
        "k": _w((lambda *s: torch.randn(*s, dtype=torch.bfloat16) * 0.8)(B, H, S, Dh)),
        "v": _w((lambda *s: torch.randn(*s, dtype=torch.bfloat16) * 0.05)(B, H, S, Dh)),
    }

def reference(q, k, v):
    scale = q.shape[-1] ** -0.5
    s = torch.einsum('bhqd,bhkd->bhqk', q, k) * scale
    p = _softmax(s, axis=-1)
    return torch.einsum('bhqk,bhkd->bhqd', p, v)
```

```cpp
#include <hip/hip_runtime.h>
#include <hip/hip_bf16.h>
#include <stdint.h>

typedef short    bf16x8 __attribute__((ext_vector_type(8)));
typedef short    s16x4  __attribute__((ext_vector_type(4)));
typedef float    f32x16 __attribute__((ext_vector_type(16)));
typedef float    f32x2  __attribute__((ext_vector_type(2)));
typedef __bf16   bf16x2 __attribute__((ext_vector_type(2)));
typedef unsigned u32x4  __attribute__((ext_vector_type(4)));
typedef unsigned u32x2  __attribute__((ext_vector_type(2)));
typedef __attribute__((address_space(3))) const char* lds_cptr;
typedef __attribute__((address_space(3))) char* lds_ptr;

#define DI __device__ __forceinline__
#define MFMA(a, b, c) __builtin_amdgcn_mfma_f32_32x32x16_bf16((a), (b), (c), 0, 0, 0)

#define QT 2
#define UNR 4
constexpr int SEQ = 2048, DH = 16, NHEADS = 64, NT = SEQ / 32;
constexpr int NW = 16 / QT;
constexpr int LDS_K = 0, LDS_V = 65536, LDS_C = 131072, LDS_BYTES = LDS_C + UNR * 1024;
constexpr int ROUNDS = 128 / NW;
constexpr int TPR = NW / 2;

DI unsigned cvtpk(float lo, float hi) { f32x2 v = {lo, hi}; bf16x2 b = __builtin_convertvector(v, bf16x2); return __builtin_bit_cast(unsigned, b); }
DI void glds16(const void* g, unsigned lds_base) {
    unsigned sv; asm volatile("s_mov_b32 %0, m0\n\ts_mov_b32 m0, %2\n\ts_nop 0\n\tglobal_load_lds_dwordx4 %1, off\n\ts_mov_b32 m0, %0" : "=&s"(sv) : "v"(g), "s"(lds_base) : "memory"); }
DI s16x4 vtr(lds_cptr p) { return __builtin_bit_cast(s16x4, __builtin_amdgcn_ds_read_tr16_b64_v4i16((__attribute__((address_space(3))) s16x4*)p)); }
DI bf16x8 kld(lds_cptr p) { return *(const __attribute__((address_space(3))) bf16x8*)p; }
DI float bf_lo(unsigned u) { return __uint_as_float(u << 16); }
DI float bf_hi(unsigned u) { return __uint_as_float(u & 0xffff0000u); }
#define MX3(a, b, c) __builtin_fmaxf(__builtin_fmaxf((a), (b)), (c))
#define WAIT_BAR(N) asm volatile("s_waitcnt vmcnt(%0) lgkmcnt(0)\n\ts_barrier" :: "i"(N) : "memory")

__global__ __launch_bounds__(64 * NW, 1) void attn_d16_kernel(const uint16_t* __restrict__ Q, const uint16_t* __restrict__ K, const uint16_t* __restrict__ V, uint16_t* __restrict__ O) {
    extern __shared__ __attribute__((aligned(16))) char lds[];
    const int tid = threadIdx.x, lane = tid & 63, r32 = lane & 31, h = lane >> 5;
    const int wid = __builtin_amdgcn_readfirstlane(tid >> 6);
    const int bid = blockIdx.x, head = (bid & 7) * 8 + (bid >> 5), quarter = (bid >> 3) & 3;
    const char* Kh = (const char*)K + (size_t)head * (SEQ * DH * 2);
    const char* Vh = (const char*)V + (size_t)head * (SEQ * DH * 2);
    const size_t qrow0 = (size_t)head * SEQ + quarter * 512 + wid * (32 * QT);
    const unsigned lds0 = (unsigned)(uintptr_t)lds;

    u32x4 qraw[QT];
#pragma unroll
    for (int a = 0; a < QT; ++a) qraw[a] = *(const u32x4*)((const char*)Q + (qrow0 + a * 32 + r32) * 32 + h * 16);
    {
        const int isv = wid & 1, tile0 = wid >> 1;
        const char* src = isv ? Vh + lane * 16 : Kh + r32 * 32 + h * 16;
        src += tile0 * 1024;
        const unsigned dst = lds0 + (isv ? LDS_V : LDS_K) + tile0 * 1024;
#pragma unroll
        for (int i = 0; i < ROUNDS; ++i) glds16(src + i * TPR * 1024, dst + i * TPR * 1024);
    }
    for (int i = tid; i < UNR * 256; i += 64 * NW) *(__attribute__((address_space(3))) unsigned*)((lds_ptr)lds + LDS_C + i * 4) = 0x3f803f80u;

    constexpr float C2 = 0.25f * 1.4426950408889634f;
    bf16x8 qf[QT];
#pragma unroll
    for (int a = 0; a < QT; ++a) {
        u32x4 t;
#pragma unroll
        for (int j = 0; j < 4; ++j) t[j] = cvtpk(bf_lo(qraw[a][j]) * C2, bf_hi(qraw[a][j]) * C2);
        qf[a] = __builtin_bit_cast(bf16x8, t);
    }
    const lds_cptr kp0 = (lds_cptr)lds + LDS_K + lane * 16;
    const int g = lane >> 4, isc = g & 1, i16 = lane & 15;
    const lds_cptr vp0 = (lds_cptr)lds + (isc ? LDS_C + (h ^ 1) * 128 : LDS_V + h * 128) + i16 * 8;
    const int vinc = isc ? 0 : UNR * 1024;

    WAIT_BAR(ROUNDS - 1);
    f32x16 o[QT];
    int nmax = 1;
    for (int pass = 0; pass < 2; ++pass) {
        f32x16 negm[QT];
#pragma unroll
        for (int a = 0; a < QT; ++a) {
            float m = -INFINITY;
            for (int t = 0; t < nmax; ++t) {
                const f32x16 s = MFMA(kld(kp0 + t * 1024), qf[a], f32x16{});
#pragma unroll
                for (int r = 0; r < 16; r += 2) m = MX3(m, s[r], s[r + 1]);
            }
            auto rr = __builtin_amdgcn_permlane32_swap(__float_as_uint(m), __float_as_uint(m), false, false);
            m = -__builtin_fmaxf(__uint_as_float(rr[0]), __uint_as_float(rr[1]));
#pragma unroll
            for (int r = 0; r < 16; ++r) negm[a][r] = m;
            asm volatile("" : "+v"(negm[a]));
            o[a] = f32x16{};
        }
        lds_cptr kp = kp0, vp = vp0;
        for (int it = 0; it < NT / UNR; ++it) {
            if (pass == 0) {
                if (it * UNR == TPR) WAIT_BAR(ROUNDS - 3);
                else if (it * UNR == 3 * TPR) WAIT_BAR(NW == 16 ? 0 : ROUNDS - 6);
                else if (NW != 16 && it * UNR == 6 * TPR) WAIT_BAR(0);
            }
#pragma unroll
            for (int u = 0; u < UNR; ++u) {
                const bf16x8 kf = kld(kp + u * 1024);
                const s16x4 v0 = vtr(vp + u * 1024), v1 = vtr(vp + u * 1024 + 256), v2 = vtr(vp + u * 1024 + 512), v3 = vtr(vp + u * 1024 + 768);
                const bf16x8 vf0 = __builtin_shufflevector(v0, v1, 0, 1, 2, 3, 4, 5, 6, 7), vf1 = __builtin_shufflevector(v2, v3, 0, 1, 2, 3, 4, 5, 6, 7);
#pragma unroll
                for (int a = 0; a < QT; ++a) {
                    f32x16 s = MFMA(kf, qf[a], negm[a]);
#pragma unroll
                    for (int r = 0; r < 16; ++r) s[r] = __builtin_amdgcn_exp2f(s[r]);
                    u32x4 p0, p1;
#pragma unroll
                    for (int j = 0; j < 4; ++j) { p0[j] = cvtpk(s[2 * j], s[2 * j + 1]); p1[j] = cvtpk(s[8 + 2 * j], s[9 + 2 * j]); }
                    o[a] = MFMA(vf0, __builtin_bit_cast(bf16x8, p0), o[a]);
                    o[a] = MFMA(vf1, __builtin_bit_cast(bf16x8, p1), o[a]);
                }
            }
            kp += UNR * 1024; vp += vinc;
        }
        bool bad = false;
#pragma unroll
        for (int a = 0; a < QT; ++a) {
            const float l = o[a][8];
            bad |= !(l <= 0x1p64f);
            const float rl = __builtin_amdgcn_rcpf(l);
#pragma unroll
            for (int r = 0; r < 8; ++r) { bad |= !(__builtin_fabsf(o[a][r]) <= 0x1p126f); o[a][r] *= rl; }
        }
        if (!__any(bad)) break;
        nmax = NT;
    }
#pragma unroll
    for (int a = 0; a < QT; ++a) {
        unsigned a0 = cvtpk(o[a][0], o[a][1]), a1 = cvtpk(o[a][2], o[a][3]), b0 = cvtpk(o[a][4], o[a][5]), b1 = cvtpk(o[a][6], o[a][7]);
        auto r0 = __builtin_amdgcn_permlane32_swap(a0, b0, false, false);
        auto r1 = __builtin_amdgcn_permlane32_swap(a1, b1, false, false);
        u32x4 w = {r0[0], r1[0], r0[1], r1[1]};
        *(u32x4*)((char*)O + (qrow0 + a * 32 + r32) * 32 + h * 16) = w;
    }
}

extern "C" void kernel_launch(void* const* d_in, const int* in_sizes, int n_in, void* d_out, int out_size, void* d_ws, size_t ws_size, hipStream_t stream) {
    (void)hipFuncSetAttribute((const void*)attn_d16_kernel, hipFuncAttributeMaxDynamicSharedMemorySize, LDS_BYTES);
    attn_d16_kernel<<<256, 64 * NW, LDS_BYTES, stream>>>((const uint16_t*)d_in[0], (const uint16_t*)d_in[1], (const uint16_t*)d_in[2], (uint16_t*)d_out);
}
```

```cpp
#include <hip/hip_runtime.h>
#include <stdint.h>
#define LDSB 0
#define NBLK 1024
#define NTHR (131072 / NBLK > 1024 ? 512 : 131072 / NBLK)
#define DELAY 100000
typedef unsigned u32x4 __attribute__((ext_vector_type(4)));
__device__ inline float lo(unsigned u) { return __uint_as_float(u << 16); }
__device__ inline float hi(unsigned u) { return __uint_as_float(u & 0xffff0000u); }
__global__ __launch_bounds__(512) void naive(const u32x4* __restrict__ Q, const u32x4* __restrict__ K, const u32x4* __restrict__ V, u32x4* __restrict__ O) {
    unsigned long long rt0; asm volatile("s_memrealtime %0\n\ts_waitcnt lgkmcnt(0)" : "=s"(rt0) :: "memory"); __builtin_amdgcn_sched_barrier(0);
    const int row = blockIdx.x * blockDim.x + threadIdx.x, head = __builtin_amdgcn_readfirstlane(row >> 11);
    float q[16], o[16]; u32x4 a = Q[row * 2], b = Q[row * 2 + 1];
    for (int j = 0; j < 4; ++j) { q[2 * j] = lo(a[j]); q[2 * j + 1] = hi(a[j]); q[8 + 2 * j] = lo(b[j]); q[9 + 2 * j] = hi(b[j]); }
    for (int j = 0; j < 16; ++j) o[j] = 0;
    float m = -INFINITY, l = 0;
    for (int k = 0; k < 2048; ++k) {
        const u32x4 c = K[(head * 2048 + k) * 2], d = K[(head * 2048 + k) * 2 + 1], e = V[(head * 2048 + k) * 2], f = V[(head * 2048 + k) * 2 + 1];
        float s = 0; for (int j = 0; j < 4; ++j) s += q[2 * j] * lo(c[j]) + q[2 * j + 1] * hi(c[j]) + q[8 + 2 * j] * lo(d[j]) + q[9 + 2 * j] * hi(d[j]);
        s *= 0.25f * 1.44269504f; const float mn = fmaxf(m, s), al = exp2f(m - mn), p = exp2f(s - mn); m = mn; l = l * al + p;
        for (int j = 0; j < 4; ++j) { o[2 * j] = o[2 * j] * al + p * lo(e[j]); o[2 * j + 1] = o[2 * j + 1] * al + p * hi(e[j]); o[8 + 2 * j] = o[8 + 2 * j] * al + p * lo(f[j]); o[9 + 2 * j] = o[9 + 2 * j] * al + p * hi(f[j]); }
    }
    u32x4 w[2]; for (int j = 0; j < 8; ++j) { unsigned x = __float_as_uint(o[2 * j] / l), y = __float_as_uint(o[2 * j + 1] / l); x += 0x7fff + ((x >> 16) & 1); y += 0x7fff + ((y >> 16) & 1); w[j >> 2][j & 3] = (x >> 16) | (y & 0xffff0000u); }
    O[row * 2] = w[0]; O[row * 2 + 1] = w[1];
    __builtin_amdgcn_sched_barrier(0); while (__builtin_amdgcn_s_memrealtime() - rt0 < DELAY) __builtin_amdgcn_s_sleep(2);
}
extern "C" void kernel_launch(void* const* d_in, const int* in_sizes, int n_in, void* d_out, int out_size, void* d_ws, size_t ws_size, hipStream_t stream) {
    if (LDSB) (void)hipFuncSetAttribute((const void*)naive, hipFuncAttributeMaxDynamicSharedMemorySize, LDSB);
    naive<<<NBLK, 131072 / NBLK, LDSB, stream>>>((const u32x4*)d_in[0], (const u32x4*)d_in[1], (const u32x4*)d_in[2], (u32x4*)d_out);
}
```

```cpp
#include <hip/hip_runtime.h>

constexpr int NTHREADS = 576, LDS_BYTES = 131072 + NTHREADS * 8;

__global__ __launch_bounds__(NTHREADS, 1) void attn_d16_kernel(const void* q, const void* k, const void* v, void* o) {
    asm volatile(
        "s_load_dwordx8 s[8:15], %[karg], 0\n\t"
        "v_readfirstlane_b32 s16, %[tid]\n\t"
        "v_and_b32 v5, 63, %[tid]\n\t"
        "s_and_b32 s17, %[bid], 7\n\t"
        "s_lshr_b32 s18, %[bid], 5\n\t"
        "v_and_b32 v16, 31, %[tid]\n\t"
        "v_bfe_u32 v17, %[tid], 5, 1\n\t"
        "s_lshl_b32 s17, s17, 19\n\t"
        "s_lshl_b32 s18, s18, 16\n\t"
        "v_lshlrev_b32 v16, 5, v16\n\t"
        "s_add_u32 s17, s17, s18\n\t"
        "s_lshl_b32 s18, %[bid], 11\n\t"
        "v_lshl_or_b32 v4, v17, 4, v16\n\t"
        "s_and_b32 s18, s18, 0xc000\n\t"
        "s_lshl_b32 s19, s16, 5\n\t"
        "v_lshlrev_b32 v5, 4, v5\n\t"
        "s_add_u32 s18, s18, s17\n\t"
        "s_add_u32 s18, s18, s19\n\t"
        "s_sub_u32 s18, s18, 0x800\n\t"
        "v_mov_b32 v7, 135428\n\t"
        "v_and_b32 v18, 2, %[tid]\n\t"
        "v_mov_b32 v19, 0\n\t"
        "v_cmp_eq_u32 vcc, 0, v18\n\t"
        "v_mov_b32 v18, 0x3f80\n\t"
        "v_lshlrev_b32 v12, 3, %[tid]\n\t"
        "v_cndmask_b32 v18, 0, v18, vcc\n\t"
        "v_add_u32 v12, 131072, v12\n\t"
        "s_nop 0\n\t"
        "ds_write_b64 v12, v[18:19]\n\t"
        "s_setprio 3\n\t"
        "s_waitcnt lgkmcnt(0)\n\t"
        "s_add_u32 s10, s10, s17\n\t"
        "s_addc_u32 s11, s11, 0\n\t"
        "s_cmp_eq_u32 s16, 0\n\t"
        "s_cbranch_scc1 LOADER%=\n\t"
        "s_add_u32 s8, s8, s18\n\t"
        "s_addc_u32 s9, s9, 0\n\t"
        "s_cmp_ge_u32 s16, 320\n\t"
        "s_cbranch_scc1 MAIN%=\n\t"
        "global_load_dwordx4 v[132:135], v4, s[8:9]\n\t"
        "global_load_dwordx4 v[124:127], v4, s[10:11]\n\t"
        "global_load_dwordx4 v[136:139], v4, s[8:9] offset:1024\n\t"
        "s_branch MAIN%=\n\t"
        "LOADER%=:\n\t"
        "s_add_u32 s12, s12, s17\n\t"
        "s_addc_u32 s13, s13, 0\n\t"
        "s_mov_b32 s20, 0\n\t"
        "s_mov_b32 s21, m0\n\t"
        "v_mov_b32 v12, 0\n\t"
        "LA%=:\n\t"
        "s_mov_b32 m0, s20\n\t"
        "s_add_u32 s22, s20, 0x10000\n\t"
        "global_load_lds_dwordx4 v4, s[10:11] offset:0\n\t"
        "global_load_lds_dwordx4 v4, s[10:11] offset:1024\n\t"
        "global_load_lds_dwordx4 v4, s[10:11] offset:2048\n\t"
        "global_load_lds_dwordx4 v4, s[10:11] offset:3072\n\t"
        "s_mov_b32 m0, s22\n\t"
        "s_add_u32 s20, s20, 0x1000\n\t"
        "global_load_lds_dwordx4 v5, s[12:13] offset:0\n\t"
        "global_load_lds_dwordx4 v5, s[12:13] offset:1024\n\t"
        "global_load_lds_dwordx4 v5, s[12:13] offset:2048\n\t"
        "global_load_lds_dwordx4 v5, s[12:13] offset:3072\n\t"
        "s_add_u32 s10, s10, 0x1000\n\t"
        "s_addc_u32 s11, s11, 0\n\t"
        "s_add_u32 s12, s12, 0x1000\n\t"
        "s_addc_u32 s13, s13, 0\n\t"
        "s_cmp_lg_u32 s20, 0x1000\n\t"
        "s_cbranch_scc1 LB%=\n\t"
        "s_barrier\n\t"
        "s_waitcnt vmcnt(0)\n\t"
        "v_add_u32 v12, 1, v12\n\t"
        "s_nop 0\n\t"
        "ds_write_b32 v7, v12\n\t"
        "LB%=:\n\t"
        "s_cmp_lt_u32 s20, 0x3000\n\t"
        "s_cbranch_scc1 LA%=\n\t"
        "s_waitcnt vmcnt(8)\n\t"
        "v_add_u32 v12, 1, v12\n\t"
        "s_cmp_lt_u32 s20, 0x10000\n\t"
        "ds_write_b32 v7, v12\n\t"
        "s_cbranch_scc1 LA%=\n\t"
        "s_waitcnt vmcnt(0)\n\t"
        "v_add_u32 v12, 48, v12\n\t"
        "s_nop 0\n\t"
        "ds_write_b32 v7, v12\n\t"
        "s_mov_b32 m0, s21\n\t"
        "s_waitcnt lgkmcnt(0)\n\t"
        "s_branch END%=\n\t"
        "MAIN%=:\n\t"
        "v_and_b32 v16, 16, %[tid]\n\t"
        "v_cmp_eq_u32 vcc, 0, v16\n\t"
        "v_lshlrev_b32 v17, 8, v17\n\t"
        "v_mov_b32 v8, 131200\n\t"
        "v_add_u32 v17, 65536, v17\n\t"
        "v_mov_b32 v6, 0x400\n\t"
        "v_and_b32 v16, 15, %[tid]\n\t"
        "v_cndmask_b32 v8, v8, v17, vcc\n\t"
        "v_cndmask_b32 v6, 0, v6, vcc\n\t"
        "v_and_b32 v17, 2, %[tid]\n\t"
        "v_cndmask_b32 v16, v17, v16, vcc\n\t"
        "v_lshl_add_u32 v8, v16, 3, v8\n\t"
        "s_mov_b32 s26, 0x3eb8aa3b\n\t"
        "s_mov_b32 s28, 1\n\t"
        "v_mov_b32 v11, 0xdddd\n\t"
        "s_add_u32 s14, s14, s18\n\t"
        "s_addc_u32 s15, s15, 0\n\t"
        "v_mov_b64 v[20:21], 0\n\t"
        "v_mov_b64 v[22:23], 0\n\t"
        "v_mov_b64 v[24:25], 0\n\t"
        "v_mov_b64 v[26:27], 0\n\t"
        "v_mov_b64 v[28:29], 0\n\t"
        "v_mov_b64 v[30:31], 0\n\t"
        "v_mov_b64 v[32:33], 0\n\t"
        "v_mov_b64 v[34:35], 0\n\t"
        "v_mov_b64 v[36:37], 0\n\t"
        "v_mov_b64 v[38:39], 0\n\t"
        "v_mov_b64 v[40:41], 0\n\t"
        "v_mov_b64 v[42:43], 0\n\t"
        "v_mov_b64 v[44:45], 0\n\t"
        "v_mov_b64 v[46:47], 0\n\t"
        "v_mov_b64 v[48:49], 0\n\t"
        "v_mov_b64 v[50:51], 0\n\t"
        "v_add_u32 v9, 1024, v5\n\t"
        "v_mov_b32 v10, v8\n\t"
        "s_cmp_ge_u32 s16, 320\n\t"
        "s_barrier\n\t"
        "s_cbranch_scc0 SL%=\n\t"
        "s_sleep 3\n\t"
        "global_load_dwordx4 v[132:135], v4, s[8:9]\n\t"
        "global_load_dwordx4 v[124:127], v4, s[10:11]\n\t"
        "global_load_dwordx4 v[136:139], v4, s[8:9] offset:1024\n\t"
        "SL%=:\n\t"
        "s_waitcnt vmcnt(2)\n\t"
        "v_lshlrev_b32 v140, 16, v132\n\t"
        "v_and_b32 v141, 0xffff0000, v132\n\t"
        "v_lshlrev_b32 v142, 16, v133\n\t"
        "v_and_b32 v143, 0xffff0000, v133\n\t"
        "v_lshlrev_b32 v144, 16, v134\n\t"
        "v_and_b32 v145, 0xffff0000, v134\n\t"
        "v_lshlrev_b32 v146, 16, v135\n\t"
        "v_and_b32 v147, 0xffff0000, v135\n\t"
        "v_pk_mul_f32 v[140:141], v[140:141], s[26:27] op_sel_hi:[1,0]\n\t"
        "v_pk_mul_f32 v[142:143], v[142:143], s[26:27] op_sel_hi:[1,0]\n\t"
        "v_pk_mul_f32 v[144:145], v[144:145], s[26:27] op_sel_hi:[1,0]\n\t"
        "v_pk_mul_f32 v[146:147], v[146:147], s[26:27] op_sel_hi:[1,0]\n\t"
        "v_cvt_pk_bf16_f32 v116, v140, v141\n\t"
        "v_cvt_pk_bf16_f32 v117, v142, v143\n\t"
        "v_cvt_pk_bf16_f32 v118, v144, v145\n\t"
        "v_cvt_pk_bf16_f32 v119, v146, v147\n\t"
        "s_waitcnt vmcnt(1)\n\t"
        "s_nop 0\n\t"
        "v_mfma_f32_32x32x16_bf16 v[84:99], v[124:127], v[116:119], 0\n\t"
        "s_waitcnt vmcnt(0)\n\t"
        "v_lshlrev_b32 v140, 16, v136\n\t"
        "v_and_b32 v141, 0xffff0000, v136\n\t"
        "v_lshlrev_b32 v142, 16, v137\n\t"
        "v_and_b32 v143, 0xffff0000, v137\n\t"
        "v_lshlrev_b32 v144, 16, v138\n\t"
        "v_and_b32 v145, 0xffff0000, v138\n\t"
        "v_lshlrev_b32 v146, 16, v139\n\t"
        "v_and_b32 v147, 0xffff0000, v139\n\t"
        "v_pk_mul_f32 v[140:141], v[140:141], s[26:27] op_sel_hi:[1,0]\n\t"
        "v_pk_mul_f32 v[142:143], v[142:143], s[26:27] op_sel_hi:[1,0]\n\t"
        "v_pk_mul_f32 v[144:145], v[144:145], s[26:27] op_sel_hi:[1,0]\n\t"
        "v_pk_mul_f32 v[146:147], v[146:147], s[26:27] op_sel_hi:[1,0]\n\t"
        "v_cvt_pk_bf16_f32 v120, v140, v141\n\t"
        "v_cvt_pk_bf16_f32 v121, v142, v143\n\t"
        "v_cvt_pk_bf16_f32 v122, v144, v145\n\t"
        "v_cvt_pk_bf16_f32 v123, v146, v147\n\t"
        "s_nop 1\n\t"
        "v_mfma_f32_32x32x16_bf16 v[100:115], v[124:127], v[120:123], 0\n\t"
        "v_max3_f32 v140, v84, v85, v86\n\t"
        "v_max3_f32 v141, v87, v88, v89\n\t"
        "v_max3_f32 v142, v90, v91, v92\n\t"
        "v_max3_f32 v143, v93, v94, v95\n\t"
        "v_max3_f32 v144, v96, v97, v98\n\t"
        "v_max3_f32 v140, v140, v141, v99\n\t"
        "v_max3_f32 v142, v142, v143, v144\n\t"
        "v_max_f32 v14, v140, v142\n\t"
        "v_floor_f32 v14, v14\n\t"
        "v_subrev_f32 v14, 0x42180000, v14\n\t"
        "v_mov_b32 v16, v14\n\t"
        "v_mov_b32 v17, v14\n\t"
        "s_nop 1\n\t"
        "v_permlane32_swap_b32 v16, v17\n\t"
        "v_min_f32 v52, -v16, -v17\n\t"
        "v_min_f32 v53, -v16, -v17\n\t"
        "v_mov_b64 v[54:55], v[52:53]\n\t"
        "v_mov_b64 v[56:57], v[52:53]\n\t"
        "v_mov_b64 v[58:59], v[52:53]\n\t"
        "v_mov_b64 v[60:61], v[52:53]\n\t"
        "v_mov_b64 v[62:63], v[52:53]\n\t"
        "v_mov_b64 v[64:65], v[52:53]\n\t"
        "v_mov_b64 v[66:67], v[52:53]\n\t"
        "s_mov_b32 s20, 0\n\t"
        "s_mov_b32 s22, 8\n\t"
        "v_mfma_f32_32x32x16_bf16 v[84:99], v[124:127], v[116:119], v[52:67]\n\t"
        "v_max3_f32 v140, v100, v101, v102\n\t"
        "v_max3_f32 v141, v103, v104, v105\n\t"
        "v_max3_f32 v142, v106, v107, v108\n\t"
        "v_max3_f32 v143, v109, v110, v111\n\t"
        "v_max3_f32 v144, v112, v113, v114\n\t"
        "v_max3_f32 v140, v140, v141, v115\n\t"
        "v_max3_f32 v142, v142, v143, v144\n\t"
        "v_max_f32 v15, v140, v142\n\t"
        "v_floor_f32 v15, v15\n\t"
        "v_subrev_f32 v15, 0x42180000, v15\n\t"
        "PASS%=:\n\t"
        "v_mov_b32 v16, v15\n\t"
        "v_mov_b32 v17, v15\n\t"
        "s_nop 1\n\t"
        "v_permlane32_swap_b32 v16, v17\n\t"
        "v_min_f32 v68, -v16, -v17\n\t"
        "v_min_f32 v69, -v16, -v17\n\t"
        "v_mov_b64 v[70:71], v[68:69]\n\t"
        "v_mov_b64 v[72:73], v[68:69]\n\t"
        "v_mov_b64 v[74:75], v[68:69]\n\t"
        "v_mov_b64 v[76:77], v[68:69]\n\t"
        "v_mov_b64 v[78:79], v[68:69]\n\t"
        "v_mov_b64 v[80:81], v[68:69]\n\t"
        "v_mov_b64 v[82:83], v[68:69]\n\t"
        "ds_read_b32 v12, v7\n\t"
        "s_mov_b32 s23, 1\n\t"
        "v_mfma_f32_32x32x16_bf16 v[100:115], v[124:127], v[120:123], v[68:83]\n\t"
        "v_exp_f32 v84, v84\n\t"
        "v_exp_f32 v85, v85\n\t"
        "v_exp_f32 v86, v86\n\t"
        "v_exp_f32 v87, v87\n\t"
        "v_exp_f32 v88, v88\n\t"
        "v_exp_f32 v89, v89\n\t"
        "v_exp_f32 v90, v90\n\t"
        "v_exp_f32 v91, v91\n\t"
        "v_exp_f32 v92, v92\n\t"
        "v_exp_f32 v93, v93\n\t"
        "v_exp_f32 v94, v94\n\t"
        "v_exp_f32 v95, v95\n\t"
        "v_exp_f32 v96, v96\n\t"
        "v_exp_f32 v97, v97\n\t"
        "v_exp_f32 v98, v98\n\t"
        "v_exp_f32 v99, v99\n\t"
        "s_waitcnt lgkmcnt(0)\n\t"
        "v_readfirstlane_b32 s21, v12\n\t"
        "s_nop 0\n\t"
        "s_cmp_ge_u32 s21, 1\n\t"
        "s_cbranch_scc1 PG%=\n\t"
        "PW%=:\n\t"
        "ds_read_b32 v12, v7\n\t"
        "s_waitcnt lgkmcnt(0)\n\t"
        "v_readfirstlane_b32 s21, v12\n\t"
        "s_nop 0\n\t"
        "s_cmp_ge_u32 s21, s23\n\t"
        "s_cbranch_scc0 PW%=\n\t"
        "PG%=:\n\t"
        "s_sub_u32 s21, s21, 1\n\t"
        "ds_read_b64_tr_b16 v[132:133], v10 offset:0\n\t"
        "ds_read_b64_tr_b16 v[134:135], v10 offset:128\n\t"
        "ds_read_b64_tr_b16 v[136:137], v10 offset:512\n\t"
        "ds_read_b64_tr_b16 v[138:139], v10 offset:640\n\t"
        "v_add_u32 v10, v6, v10\n\t"
        "ds_read_b128 v[128:131], v9 offset:0\n\t"
        "ds_read_b64_tr_b16 v[140:141], v10 offset:0\n\t"
        "ds_read_b64_tr_b16 v[142:143], v10 offset:128\n\t"
        "ds_read_b64_tr_b16 v[144:145], v10 offset:512\n\t"
        "ds_read_b64_tr_b16 v[146:147], v10 offset:640\n\t"
        "v_exp_f32 v100, v100\n\t"
        "v_exp_f32 v101, v101\n\t"
        "v_exp_f32 v102, v102\n\t"
        "v_exp_f32 v103, v103\n\t"
        "v_exp_f32 v104, v104\n\t"
        "s_waitcnt lgkmcnt(7)\n\t"
        "v_smfmac_f32_32x32x32_bf16 v[20:35], v[132:135], v[84:91], v11\n\t"
        "s_waitcnt lgkmcnt(0)\n\t"
        "v_smfmac_f32_32x32x32_bf16 v[20:35], v[136:139], v[92:99], v11\n\t"
        "s_branch ENTRY%=\n\t"
        ".p2align 6\n\t"
        "LOOP%=:\n\t"
        "v_exp_f32 v84, v84\n\t"
        "v_exp_f32 v85, v85\n\t"
        "v_smfmac_f32_32x32x32_bf16 v[36:51], v[144:147], v[108:115], v11\n\t"
        "ds_read_b128 v[128:131], v9 offset:0\n\t"
        "ds_read_b64_tr_b16 v[140:141], v10 offset:0\n\t"
        "ds_read_b64_tr_b16 v[142:143], v10 offset:128\n\t"
        "v_exp_f32 v86, v86\n\t"
        "v_exp_f32 v87, v87\n\t"
        "v_exp_f32 v88, v88\n\t"
        "v_mfma_f32_32x32x16_bf16 v[100:115], v[124:127], v[120:123], v[68:83]\n\t"
        "ds_read_b64_tr_b16 v[144:145], v10 offset:512\n\t"
        "ds_read_b64_tr_b16 v[146:147], v10 offset:640\n\t"
        "v_exp_f32 v89, v89\n\t"
        "v_exp_f32 v90, v90\n\t"
        "v_exp_f32 v91, v91\n\t"
        "v_exp_f32 v92, v92\n\t"
        "v_exp_f32 v93, v93\n\t"
        "v_exp_f32 v94, v94\n\t"
        "v_smfmac_f32_32x32x32_bf16 v[20:35], v[132:135], v[84:91], v11\n\t"
        "v_exp_f32 v95, v95\n\t"
        "v_exp_f32 v96, v96\n\t"
        "v_exp_f32 v97, v97\n\t"
        "v_exp_f32 v98, v98\n\t"
        "v_exp_f32 v99, v99\n\t"
        "v_exp_f32 v100, v100\n\t"
        "v_exp_f32 v101, v101\n\t"
        "v_smfmac_f32_32x32x32_bf16 v[20:35], v[136:139], v[92:99], v11\n\t"
        "s_waitcnt lgkmcnt(0)\n\t"
        "v_exp_f32 v102, v102\n\t"
        "v_exp_f32 v103, v103\n\t"
        "v_exp_f32 v104, v104\n\t"
        "ENTRY%=:\n\t"
        "v_mfma_f32_32x32x16_bf16 v[84:99], v[128:131], v[116:119], v[52:67]\n\t"
        "v_exp_f32 v105, v105\n\t"
        "v_exp_f32 v106, v106\n\t"
        "v_exp_f32 v107, v107\n\t"
        "v_exp_f32 v108, v108\n\t"
        "v_exp_f32 v109, v109\n\t"
        "v_exp_f32 v110, v110\n\t"
        "v_smfmac_f32_32x32x32_bf16 v[36:51], v[132:135], v[100:107], v11\n\t"
        "v_exp_f32 v111, v111\n\t"
        "v_exp_f32 v112, v112\n\t"
        "v_exp_f32 v113, v113\n\t"
        "v_exp_f32 v114, v114\n\t"
        "v_exp_f32 v115, v115\n\t"
        "v_exp_f32 v84, v84\n\t"
        "v_exp_f32 v85, v85\n\t"
        "v_smfmac_f32_32x32x32_bf16 v[36:51], v[136:139], v[108:115], v11\n\t"
        "ds_read_b128 v[124:127], v9 offset:1024\n\t"
        "ds_read_b64_tr_b16 v[132:133], v10 offset:1024\n\t"
        "ds_read_b64_tr_b16 v[134:135], v10 offset:1152\n\t"
        "v_exp_f32 v86, v86\n\t"
        "v_exp_f32 v87, v87\n\t"
        "v_exp_f32 v88, v88\n\t"
        "v_mfma_f32_32x32x16_bf16 v[100:115], v[128:131], v[120:123], v[68:83]\n\t"
        "ds_read_b64_tr_b16 v[136:137], v10 offset:1536\n\t"
        "ds_read_b64_tr_b16 v[138:139], v10 offset:1664\n\t"
        "v_exp_f32 v89, v89\n\t"
        "v_exp_f32 v90, v90\n\t"
        "v_exp_f32 v91, v91\n\t"
        "v_exp_f32 v92, v92\n\t"
        "v_exp_f32 v93, v93\n\t"
        "v_exp_f32 v94, v94\n\t"
        "v_smfmac_f32_32x32x32_bf16 v[20:35], v[140:143], v[84:91], v11\n\t"
        "v_exp_f32 v95, v95\n\t"
        "v_exp_f32 v96, v96\n\t"
        "v_exp_f32 v97, v97\n\t"
        "v_exp_f32 v98, v98\n\t"
        "v_exp_f32 v99, v99\n\t"
        "v_exp_f32 v100, v100\n\t"
        "v_exp_f32 v101, v101\n\t"
        "v_smfmac_f32_32x32x32_bf16 v[20:35], v[144:147], v[92:99], v11\n\t"
        "s_waitcnt lgkmcnt(0)\n\t"
        "v_exp_f32 v102, v102\n\t"
        "v_exp_f32 v103, v103\n\t"
        "v_exp_f32 v104, v104\n\t"
        "v_mfma_f32_32x32x16_bf16 v[84:99], v[124:127], v[116:119], v[52:67]\n\t"
        "v_exp_f32 v105, v105\n\t"
        "v_exp_f32 v106, v106\n\t"
        "v_exp_f32 v107, v107\n\t"
        "v_exp_f32 v108, v108\n\t"
        "v_exp_f32 v109, v109\n\t"
        "v_exp_f32 v110, v110\n\t"
        "v_smfmac_f32_32x32x32_bf16 v[36:51], v[140:143], v[100:107], v11\n\t"
        "v_exp_f32 v111, v111\n\t"
        "v_exp_f32 v112, v112\n\t"
        "v_exp_f32 v113, v113\n\t"
        "v_exp_f32 v114, v114\n\t"
        "v_exp_f32 v115, v115\n\t"
        "v_exp_f32 v84, v84\n\t"
        "v_exp_f32 v85, v85\n\t"
        "v_smfmac_f32_32x32x32_bf16 v[36:51], v[144:147], v[108:115], v11\n\t"
        "ds_read_b128 v[128:131], v9 offset:2048\n\t"
        "ds_read_b64_tr_b16 v[140:141], v10 offset:2048\n\t"
        "ds_read_b64_tr_b16 v[142:143], v10 offset:2176\n\t"
        "v_exp_f32 v86, v86\n\t"
        "v_exp_f32 v87, v87\n\t"
        "v_exp_f32 v88, v88\n\t"
        "v_mfma_f32_32x32x16_bf16 v[100:115], v[124:127], v[120:123], v[68:83]\n\t"
        "ds_read_b64_tr_b16 v[144:145], v10 offset:2560\n\t"
        "ds_read_b64_tr_b16 v[146:147], v10 offset:2688\n\t"
        "v_exp_f32 v89, v89\n\t"
        "v_exp_f32 v90, v90\n\t"
        "v_exp_f32 v91, v91\n\t"
        "v_exp_f32 v92, v92\n\t"
        "v_exp_f32 v93, v93\n\t"
        "v_exp_f32 v94, v94\n\t"
        "v_smfmac_f32_32x32x32_bf16 v[20:35], v[132:135], v[84:91], v11\n\t"
        "v_exp_f32 v95, v95\n\t"
        "v_exp_f32 v96, v96\n\t"
        "v_exp_f32 v97, v97\n\t"
        "v_exp_f32 v98, v98\n\t"
        "v_exp_f32 v99, v99\n\t"
        "v_exp_f32 v100, v100\n\t"
        "v_exp_f32 v101, v101\n\t"
        "v_smfmac_f32_32x32x32_bf16 v[20:35], v[136:139], v[92:99], v11\n\t"
        "s_cmp_eq_u32 s20, s22\n\t"
        "s_cbranch_scc1 EVP%=\n\t"
        "RESP%=:\n\t"
        "s_waitcnt lgkmcnt(0)\n\t"
        "v_exp_f32 v102, v102\n\t"
        "v_exp_f32 v103, v103\n\t"
        "v_exp_f32 v104, v104\n\t"
        "v_mfma_f32_32x32x16_bf16 v[84:99], v[128:131], v[116:119], v[52:67]\n\t"
        "v_exp_f32 v105, v105\n\t"
        "v_exp_f32 v106, v106\n\t"
        "v_exp_f32 v107, v107\n\t"
        "v_exp_f32 v108, v108\n\t"
        "v_exp_f32 v109, v109\n\t"
        "v_exp_f32 v110, v110\n\t"
        "v_smfmac_f32_32x32x32_bf16 v[36:51], v[132:135], v[100:107], v11\n\t"
        "v_exp_f32 v111, v111\n\t"
        "v_exp_f32 v112, v112\n\t"
        "v_exp_f32 v113, v113\n\t"
        "v_exp_f32 v114, v114\n\t"
        "v_exp_f32 v115, v115\n\t"
        "v_exp_f32 v84, v84\n\t"
        "v_exp_f32 v85, v85\n\t"
        "v_smfmac_f32_32x32x32_bf16 v[36:51], v[136:139], v[108:115], v11\n\t"
        "s_cmp_eq_u32 s20, s21\n\t"
        "s_cbranch_scc1 EV%=\n\t"
        "RESUME%=:\n\t"
        "ds_read_b128 v[124:127], v9 offset:3072\n\t"
        "ds_read_b64_tr_b16 v[132:133], v10 offset:3072\n\t"
        "ds_read_b64_tr_b16 v[134:135], v10 offset:3200\n\t"
        "v_exp_f32 v86, v86\n\t"
        "v_exp_f32 v87, v87\n\t"
        "v_exp_f32 v88, v88\n\t"
        "v_mfma_f32_32x32x16_bf16 v[100:115], v[128:131], v[120:123], v[68:83]\n\t"
        "ds_read_b64_tr_b16 v[136:137], v10 offset:3584\n\t"
        "ds_read_b64_tr_b16 v[138:139], v10 offset:3712\n\t"
        "v_exp_f32 v89, v89\n\t"
        "v_exp_f32 v90, v90\n\t"
        "v_exp_f32 v91, v91\n\t"
        "v_exp_f32 v92, v92\n\t"
        "v_exp_f32 v93, v93\n\t"
        "v_exp_f32 v94, v94\n\t"
        "v_smfmac_f32_32x32x32_bf16 v[20:35], v[140:143], v[84:91], v11\n\t"
        "v_exp_f32 v95, v95\n\t"
        "v_exp_f32 v96, v96\n\t"
        "v_exp_f32 v97, v97\n\t"
        "v_exp_f32 v98, v98\n\t"
        "v_exp_f32 v99, v99\n\t"
        "v_exp_f32 v100, v100\n\t"
        "v_exp_f32 v101, v101\n\t"
        "v_smfmac_f32_32x32x32_bf16 v[20:35], v[144:147], v[92:99], v11\n\t"
        "s_waitcnt lgkmcnt(0)\n\t"
        "v_exp_f32 v102, v102\n\t"
        "v_exp_f32 v103, v103\n\t"
        "v_exp_f32 v104, v104\n\t"
        "v_mfma_f32_32x32x16_bf16 v[84:99], v[124:127], v[116:119], v[52:67]\n\t"
        "s_add_u32 s20, s20, 1\n\t"
        "v_exp_f32 v105, v105\n\t"
        "v_exp_f32 v106, v106\n\t"
        "v_exp_f32 v107, v107\n\t"
        "v_exp_f32 v108, v108\n\t"
        "v_exp_f32 v109, v109\n\t"
        "v_exp_f32 v110, v110\n\t"
        "v_smfmac_f32_32x32x32_bf16 v[36:51], v[140:143], v[100:107], v11\n\t"
        "s_cmp_lg_u32 s20, 16\n\t"
        "v_add_u32 v9, 4096, v9\n\t"
        "v_lshl_add_u32 v10, v6, 2, v10\n\t"
        "v_exp_f32 v111, v111\n\t"
        "v_exp_f32 v112, v112\n\t"
        "v_exp_f32 v113, v113\n\t"
        "v_exp_f32 v114, v114\n\t"
        "v_exp_f32 v115, v115\n\t"
        "s_cbranch_scc1 LOOP%=\n\t"
        "s_nop 0\n\t"
        "v_smfmac_f32_32x32x32_bf16 v[36:51], v[144:147], v[108:115], v11\n\t"
        "v_rcp_f32 v16, v28\n\t"
        "v_pk_add_f32 v[84:85], v[20:21], v[22:23]\n\t"
        "v_pk_mul_f32 v[92:93], v[20:21], v[16:17] op_sel_hi:[1,0]\n\t"
        "v_pk_mul_f32 v[94:95], v[22:23], v[16:17] op_sel_hi:[1,0]\n\t"
        "v_pk_mul_f32 v[96:97], v[24:25], v[16:17] op_sel_hi:[1,0]\n\t"
        "v_pk_mul_f32 v[98:99], v[26:27], v[16:17] op_sel_hi:[1,0]\n\t"
        "v_cvt_pk_bf16_f32 v124, v92, v93\n\t"
        "v_cvt_pk_bf16_f32 v126, v96, v97\n\t"
        "v_cvt_pk_bf16_f32 v125, v94, v95\n\t"
        "v_cvt_pk_bf16_f32 v127, v98, v99\n\t"
        "v_permlane32_swap_b32 v124, v126\n\t"
        "v_pk_add_f32 v[86:87], v[24:25], v[26:27]\n\t"
        "v_permlane32_swap_b32 v125, v127\n\t"
        "v_rcp_f32 v18, v44\n\t"
        "global_store_dwordx4 v4, v[124:127], s[14:15] sc1\n\t"
        "v_pk_mul_f32 v[92:93], v[36:37], v[18:19] op_sel_hi:[1,0]\n\t"
        "v_pk_mul_f32 v[94:95], v[38:39], v[18:19] op_sel_hi:[1,0]\n\t"
        "v_pk_mul_f32 v[96:97], v[40:41], v[18:19] op_sel_hi:[1,0]\n\t"
        "v_pk_mul_f32 v[98:99], v[42:43], v[18:19] op_sel_hi:[1,0]\n\t"
        "v_cvt_pk_bf16_f32 v128, v92, v93\n\t"
        "v_cvt_pk_bf16_f32 v130, v96, v97\n\t"
        "v_cvt_pk_bf16_f32 v129, v94, v95\n\t"
        "v_cvt_pk_bf16_f32 v131, v98, v99\n\t"
        "v_permlane32_swap_b32 v128, v130\n\t"
        "v_pk_add_f32 v[88:89], v[36:37], v[38:39]\n\t"
        "v_permlane32_swap_b32 v129, v131\n\t"
        "v_pk_add_f32 v[90:91], v[40:41], v[42:43]\n\t"
        "global_store_dwordx4 v4, v[128:131], s[14:15] offset:1024 sc1\n\t"
        "v_pk_add_f32 v[84:85], v[84:85], v[86:87]\n\t"
        "v_pk_add_f32 v[88:89], v[88:89], v[90:91]\n\t"
        "v_max_f32 v12, v28, v44\n\t"
        "s_mov_b32 s24, 0x7f000000\n\t"
        "s_mov_b32 s25, 0x72800000\n\t"
        "v_pk_add_f32 v[84:85], v[84:85], v[88:89]\n\t"
        "v_cmp_nge_f32 vcc, s25, v12\n\t"
        "v_add_f32 v84, v84, v85\n\t"
        "v_cmp_nle_f32 s[22:23], |v84|, s24\n\t"
        "s_nop 1\n\t"
        "s_or_b64 vcc, vcc, s[22:23]\n\t"
        "s_cselect_b32 s24, s28, 0\n\t"
        "s_cmp_lg_u32 s24, 0\n\t"
        "s_cbranch_scc0 END%=\n\t"
        "s_branch FALL%=\n\t"
        "EV%=:\n\t"
        "ds_read_b32 v12, v7\n\t"
        "s_waitcnt lgkmcnt(0)\n\t"
        "v_readfirstlane_b32 s21, v12\n\t"
        "s_add_u32 s23, s20, 2\n\t"
        "s_cmp_ge_u32 s21, s23\n\t"
        "s_cbranch_scc0 EV%=\n\t"
        "s_sub_u32 s21, s21, 1\n\t"
        "s_branch RESUME%=\n\t"
        "EVP%=:\n\t"
        "s_cmp_lg_u32 s20, 8\n\t"
        "s_cbranch_scc1 EVN0_%=\n\t"
        "s_setprio 2\n\t"
        "s_mov_b32 s22, 12\n\t"
        "s_branch RESP%=\n\t"
        "EVN0_%=:\n\t"
        "s_cmp_lg_u32 s20, 12\n\t"
        "s_cbranch_scc1 EVN1_%=\n\t"
        "s_setprio 1\n\t"
        "s_mov_b32 s22, 15\n\t"
        "s_branch RESP%=\n\t"
        "EVN1_%=:\n\t"
        "s_setprio 0\n\t"
        "s_mov_b32 s22, -1\n\t"
        "s_branch RESP%=\n\t"
        "FALL%=:\n\t"
        "s_waitcnt vmcnt(0)\n\t"
        "s_mov_b32 s28, 0\n\t"
        "s_mov_b32 s20, 63\n\t"
        "v_add_u32 v9, 0xfc00, v5\n\t"
        "v_mov_b64 v[20:21], 0\n\t"
        "v_mov_b64 v[22:23], 0\n\t"
        "v_mov_b64 v[24:25], 0\n\t"
        "v_mov_b64 v[26:27], 0\n\t"
        "v_mov_b64 v[28:29], 0\n\t"
        "v_mov_b64 v[30:31], 0\n\t"
        "v_mov_b64 v[32:33], 0\n\t"
        "v_mov_b64 v[34:35], 0\n\t"
        "v_mov_b64 v[36:37], 0\n\t"
        "v_mov_b64 v[38:39], 0\n\t"
        "v_mov_b64 v[40:41], 0\n\t"
        "v_mov_b64 v[42:43], 0\n\t"
        "v_mov_b64 v[44:45], 0\n\t"
        "v_mov_b64 v[46:47], 0\n\t"
        "v_mov_b64 v[48:49], 0\n\t"
        "v_mov_b64 v[50:51], 0\n\t"
        "FL%=:\n\t"
        "ds_read_b128 v[124:127], v9\n\t"
        "v_subrev_u32 v9, 0x400, v9\n\t"
        "s_waitcnt lgkmcnt(0)\n\t"
        "v_mfma_f32_32x32x16_bf16 v[84:99], v[124:127], v[116:119], 0\n\t"
        "v_mfma_f32_32x32x16_bf16 v[100:115], v[124:127], v[120:123], 0\n\t"
        "s_nop 11\n\t"
        "v_max3_f32 v14, v84, v85, v14\n\t"
        "v_max3_f32 v15, v100, v101, v15\n\t"
        "v_max3_f32 v14, v86, v87, v14\n\t"
        "v_max3_f32 v15, v102, v103, v15\n\t"
        "v_max3_f32 v14, v88, v89, v14\n\t"
        "v_max3_f32 v15, v104, v105, v15\n\t"
        "v_max3_f32 v14, v90, v91, v14\n\t"
        "v_max3_f32 v15, v106, v107, v15\n\t"
        "v_max3_f32 v14, v92, v93, v14\n\t"
        "v_max3_f32 v15, v108, v109, v15\n\t"
        "v_max3_f32 v14, v94, v95, v14\n\t"
        "v_max3_f32 v15, v110, v111, v15\n\t"
        "v_max3_f32 v14, v96, v97, v14\n\t"
        "v_max3_f32 v15, v112, v113, v15\n\t"
        "v_max3_f32 v14, v98, v99, v14\n\t"
        "v_max3_f32 v15, v114, v115, v15\n\t"
        "s_sub_u32 s20, s20, 1\n\t"
        "s_cbranch_scc0 FL%=\n\t"
        "v_add_u32 v9, 1024, v5\n\t"
        "v_mov_b32 v10, v8\n\t"
        "v_mov_b32 v16, v14\n\t"
        "v_mov_b32 v17, v14\n\t"
        "s_nop 1\n\t"
        "v_permlane32_swap_b32 v16, v17\n\t"
        "v_min_f32 v52, -v16, -v17\n\t"
        "v_min_f32 v53, -v16, -v17\n\t"
        "v_mov_b64 v[54:55], v[52:53]\n\t"
        "v_mov_b64 v[56:57], v[52:53]\n\t"
        "v_mov_b64 v[58:59], v[52:53]\n\t"
        "v_mov_b64 v[60:61], v[52:53]\n\t"
        "v_mov_b64 v[62:63], v[52:53]\n\t"
        "v_mov_b64 v[64:65], v[52:53]\n\t"
        "v_mov_b64 v[66:67], v[52:53]\n\t"
        "s_mov_b32 s20, 0\n\t"
        "s_mov_b32 s22, -1\n\t"
        "v_mfma_f32_32x32x16_bf16 v[84:99], v[124:127], v[116:119], v[52:67]\n\t"
        "s_branch PASS%=\n\t"
        "END%=:\n\t"
        :
        : [tid] "v"(threadIdx.x), [bid] "s"(blockIdx.x), [karg] "s"(__builtin_amdgcn_kernarg_segment_ptr())
        : "v4", "v5", "v6", "v7", "v8", "v9", "v10", "v11", "v12", "v13", "v14", "v15", "v16", "v17", "v18", "v19", "v20", "v21", "v22", "v23",
          "v24", "v25", "v26", "v27", "v28", "v29", "v30", "v31", "v32", "v33", "v34", "v35", "v36", "v37", "v38", "v39", "v40", "v41", "v42", "v43",
          "v44", "v45", "v46", "v47", "v48", "v49", "v50", "v51", "v52", "v53", "v54", "v55", "v56", "v57", "v58", "v59", "v60", "v61", "v62", "v63",
          "v64", "v65", "v66", "v67", "v68", "v69", "v70", "v71", "v72", "v73", "v74", "v75", "v76", "v77", "v78", "v79", "v80", "v81", "v82", "v83",
          "v84", "v85", "v86", "v87", "v88", "v89", "v90", "v91", "v92", "v93", "v94", "v95", "v96", "v97", "v98", "v99", "v100", "v101", "v102", "v103",
          "v104", "v105", "v106", "v107", "v108", "v109", "v110", "v111", "v112", "v113", "v114", "v115", "v116", "v117", "v118", "v119", "v120", "v121", "v122", "v123",
          "v124", "v125", "v126", "v127", "v128", "v129", "v130", "v131", "v132", "v133", "v134", "v135", "v136", "v137", "v138", "v139", "v140", "v141", "v142", "v143",
          "v144", "v145", "v146", "v147", "s8", "s9", "s10", "s11", "s12", "s13", "s14", "s15", "s16", "s17", "s18", "s19", "s20", "s21", "s22", "s23",
          "s24", "s25", "s26", "s27", "s28",
          "vcc", "scc", "memory");
}

extern "C" void kernel_launch(void* const* d_in, const int* in_sizes, int n_in, void* d_out, int out_size, void* d_ws, size_t ws_size, hipStream_t stream) {
    (void)hipFuncSetAttribute((const void*)attn_d16_kernel, hipFuncAttributeMaxDynamicSharedMemorySize, LDS_BYTES);
    attn_d16_kernel<<<256, NTHREADS, LDS_BYTES, stream>>>(d_in[0], d_in[1], d_in[2], d_out);
}
```
